```python
import torch

def _softmax(x, axis=-1):
    return torch.softmax(x, dim=axis)
def _gelu(x):
    return torch.nn.functional.gelu(x, approximate="tanh")
def _silu(x):
    return torch.nn.functional.silu(x)
def _sigmoid(x):
    return torch.sigmoid(x)
def _layernorm(x, axis=-1, eps=1e-5):
    m = x.mean(dim=axis, keepdim=True)
    v = x.var(dim=axis, keepdim=True, unbiased=False)
    return (x - m) / torch.sqrt(v + eps)
def _scatter_add(tgt, idx, src, axis=0):
    return tgt.clone().index_add_(axis, idx, src)
def _segment_sum(src, seg, n):
    out = torch.zeros((n,) + src.shape[1:], dtype=src.dtype)
    return out.index_add_(0, seg, src)
def _pad(x, pw):
    flat = tuple(v for pair in reversed(pw) for v in pair)
    return torch.nn.functional.pad(x, flat)

kernel_name = 'parallel_sum__mha_block_cached__glu_gated_glu__decode'

def setup_inputs():

    _w = (lambda a: (lambda t: t.to(torch.bfloat16) if t.is_floating_point() else t)(torch.as_tensor(a)))
    def _A_setup():
        B, Sq, D = 8, 1, 448
        H, Sk, Dh = 16, 4096, 256
        return {
            "x": _w((lambda *s: torch.randn(*s, dtype=torch.bfloat16) * 0.05)(B, Sq, D)),
            "k_cache": _w((lambda *s: torch.randn(*s, dtype=torch.bfloat16) * 3.2)(B, H, Sk, Dh)),
            "v_cache": _w((lambda *s: torch.randn(*s, dtype=torch.bfloat16) * 1.6)(B, H, Sk, Dh)),
            "wq": _w((lambda *s: torch.randn(*s, dtype=torch.bfloat16) * 3.2)(D, H * Dh) * (D ** -0.5)),
            "wo": _w((lambda *s: torch.randn(*s, dtype=torch.bfloat16) * 1.6)(H * Dh, D) * ((H * Dh) ** -0.5)),
        }
    _A_in = _A_setup()

    def _B_setup():
        B, S, D, F = 8, 1, 448, 1792
        return {
            "x": _w((lambda *s: torch.randn(*s, dtype=torch.bfloat16) * 0.05)(B, S, D)),
            "w_gate": _w((lambda *s: torch.randn(*s, dtype=torch.bfloat16) * 0.4)(D, F)),
            "w_up": _w((lambda *s: torch.randn(*s, dtype=torch.bfloat16) * 0.05)(D, F)),
            "w_down": _w((lambda *s: torch.randn(*s, dtype=torch.bfloat16) * 0.05)(F, D)),
        }
    _B_in = _B_setup()

    return {
        "A_x": _A_in["x"],
        "A_k_cache": _A_in["k_cache"],
        "A_v_cache": _A_in["v_cache"],
        "A_wq": _A_in["wq"],
        "A_wo": _A_in["wo"],
        "B_x": _B_in["x"],
        "B_w_gate": _B_in["w_gate"],
        "B_w_up": _B_in["w_up"],
        "B_w_down": _B_in["w_down"],
    }

def reference(A_x, A_k_cache, A_v_cache, A_wq, A_wo, B_x, B_w_gate, B_w_up, B_w_down):
    def _A_ref(x, k_cache, v_cache, wq, wo):
        B, Sq, D = x.shape
        H, Dh = 16, 256
        q = (lambda x, *d: x.permute(*d))((x @ wq).reshape(B, Sq, H, Dh), 0, 2, 1, 3)
        scores = torch.einsum("bhqd,bhkd->bhqk", q, k_cache) * (Dh ** -0.5)
        p = _softmax(scores, axis=-1)
        o = torch.einsum("bhqk,bhkd->bhqd", p, v_cache)
        return (lambda x, *d: x.permute(*d))(o, 0, 2, 1, 3).reshape(B, Sq, H * Dh) @ wo

    def _B_ref(x, w_gate, w_up, w_down):
        g = torch.einsum("bsd,df->bsf", x, w_gate)
        u = torch.einsum("bsd,df->bsf", x, w_up)
        h = _sigmoid(g) * u
        return torch.einsum("bsf,fd->bsd", h, w_down)

    x = A_x
    xn = _layernorm(x, axis=-1)
    return x + _A_ref(xn, A_k_cache, A_v_cache, A_wq, A_wo) + _B_ref(xn, B_w_gate, B_w_up, B_w_down)
```

```cpp
#include <hip/hip_runtime.h>
#include <hip/hip_bf16.h>
#include <stdint.h>

#define MIMIC 1

typedef unsigned u32x4 __attribute__((ext_vector_type(4)));
typedef __attribute__((ext_vector_type(2))) __bf16 bf16x2;
constexpr int B = 8, D = 448, H = 16, SK = 4096, DH = 256, F = 1792;
constexpr int NSPLIT = 4;

__device__ __forceinline__ float bf2f(unsigned short u) { return __uint_as_float((unsigned)u << 16); }
__device__ __forceinline__ float rbf(float f) { return __bfloat162float(__float2bfloat16(f)); }
__device__ __forceinline__ float dot2(unsigned k, unsigned q, float acc) {
  return __builtin_amdgcn_fdot2_f32_bf16(__builtin_bit_cast(bf16x2, k), __builtin_bit_cast(bf16x2, q), acc, false);
}

__device__ void layernorm_row(const unsigned short* x, float* xn  , float* red) {
  int tid = threadIdx.x;
  if (tid < 64) {
    float s = 0.f;
    float v[7];
    for (int i = 0; i < 7; ++i) { v[i] = bf2f(x[tid + 64 * i]); s += v[i]; }
    for (int o = 32; o > 0; o >>= 1) s += __shfl_xor(s, o);
    float m = s / D;
    float q = 0.f;
    for (int i = 0; i < 7; ++i) { float d = v[i] - m; q += d * d; }
    for (int o = 32; o > 0; o >>= 1) q += __shfl_xor(q, o);
    float var = q / D;
    float mb = rbf(m), vb = rbf(var);
    float den = rbf(sqrtf(rbf(vb + 1e-5f)));
    for (int i = 0; i < 7; ++i) xn[tid + 64 * i] = rbf(rbf(v[i] - mb) / den);
  }
  __syncthreads();
}

__global__ void k1(const unsigned short* x, const unsigned short* wq, const unsigned short* wg, const unsigned short* wu,
                   float* q, float* hbuf) {
  __shared__ float xn[D];
  __shared__ float red[8];
  int b = blockIdx.x;
  layernorm_row(x + b * D, xn, red);
  int c = blockIdx.y * 256 + threadIdx.x;
  if (c < H * DH) {
    float a = 0.f;
    for (int d = 0; d < D; ++d) a += xn[d] * bf2f(wq[d * (H * DH) + c]);
    q[b * H * DH + c] = rbf(a);
  } else {
    int f = c - H * DH;
    float g = 0.f, u = 0.f;
    for (int d = 0; d < D; ++d) { g += xn[d] * bf2f(wg[d * F + f]); u += xn[d] * bf2f(wu[d * F + f]); }
    g = rbf(g); u = rbf(u);
    float sg = rbf(1.f / (1.f + __expf(-g)));
    hbuf[b * F + f] = rbf(sg * u);
  }
}

__global__ void __launch_bounds__(512) k2(const unsigned short* kc, const unsigned short* vc, const float* q, float* part) {
  constexpr int KEYS = SK / NSPLIT, KW = KEYS / 8;
  __shared__ float sc[8][KW];
  __shared__ float mrg[8][DH + 2];
  int s = blockIdx.x, h = blockIdx.y, b = blockIdx.z;
  int tid = threadIdx.x, lane = tid & 63, w = tid >> 6, half = lane >> 5, c = lane & 31;
  const float* qp = q + (b * H + h) * DH + c * 8;
  unsigned qpk[4];
  for (int i = 0; i < 4; ++i) qpk[i] = (__float_as_uint(qp[2 * i]) >> 16) | (__float_as_uint(qp[2 * i + 1]) & 0xffff0000u);
  size_t base = ((size_t)(b * H + h) * SK + s * KEYS + w * KW) * DH;
  const u32x4* kp = (const u32x4*)(kc + base) + lane;
  const u32x4* vp = (const u32x4*)(vc + base) + lane;
  for (int i = 0; i < KW / 2; i += 8) {
    u32x4 kk[8];
    for (int j = 0; j < 8; ++j) kk[j] = __builtin_nontemporal_load(kp + (i + j) * 64);
    for (int j = 0; j < 8; ++j) {
      float d = 0.f;
      for (int t = 0; t < 4; ++t) d = dot2(kk[j][t], qpk[t], d);
      for (int o = 16; o > 0; o >>= 1) d += __shfl_xor(d, o);
      d = rbf(d);
      if (c == 0) sc[w][2 * (i + j) + half] = d * 0.0625f;
    }
  }
  __syncthreads();
  float m = -INFINITY;
  for (int i = lane; i < KW; i += 64) m = fmaxf(m, sc[w][i]);
  for (int o = 32; o > 0; o >>= 1) m = fmaxf(m, __shfl_xor(m, o));
  float l = 0.f;
  for (int i = lane; i < KW; i += 64) { float p = __expf(sc[w][i] - m); sc[w][i] = p; l += p; }
  for (int o = 32; o > 0; o >>= 1) l += __shfl_xor(l, o);
  __syncthreads();
  float acc[8] = {0, 0, 0, 0, 0, 0, 0, 0};
  for (int i = 0; i < KW / 2; i += 8) {
    u32x4 vv[8];
    for (int j = 0; j < 8; ++j) vv[j] = __builtin_nontemporal_load(vp + (i + j) * 64);
    for (int j = 0; j < 8; ++j) {
      float p = sc[w][2 * (i + j) + half];
      for (int t = 0; t < 4; ++t) {
        acc[2 * t] += p * __uint_as_float(vv[j][t] << 16);
        acc[2 * t + 1] += p * __uint_as_float(vv[j][t] & 0xffff0000u);
      }
    }
  }
  for (int t = 0; t < 8; ++t) acc[t] += __shfl_xor(acc[t], 32);
  if (half == 0) for (int t = 0; t < 8; ++t) mrg[w][c * 8 + t] = acc[t];
  if (lane == 0) { mrg[w][DH] = m; mrg[w][DH + 1] = l; }
  __syncthreads();
  if (tid < DH) {
    float M = -INFINITY;
    for (int i = 0; i < 8; ++i) M = fmaxf(M, mrg[i][DH]);
    float L = 0.f, o = 0.f;
    for (int i = 0; i < 8; ++i) { float e = __expf(mrg[i][DH] - M); L += e * mrg[i][DH + 1]; o += e * mrg[i][tid]; }
    float* pp = part + (size_t)((b * H + h) * NSPLIT + s) * (DH + 2);
    pp[tid] = o;
    if (tid == 0) { pp[DH] = M; pp[DH + 1] = L; }
  }
}

__global__ void __launch_bounds__(1024) k3(const unsigned short* x, const unsigned short* wo, const unsigned short* wd, const float* part,
                   const float* hbuf, __hip_bfloat16* out) {
  __shared__ float o[H * DH];
  __shared__ float red[16][2][64];
  int b = blockIdx.x, tid = threadIdx.x, lane = tid & 63, w = tid >> 6;
  for (int i = tid; i < H * DH; i += 1024) {
    int h = i / DH, d = i % DH;
    const float* pp = part + (size_t)((b * H + h) * NSPLIT) * (DH + 2);
    float M = -INFINITY;
    for (int s = 0; s < NSPLIT; ++s) M = fmaxf(M, pp[s * (DH + 2) + DH]);
    float L = 0.f, a = 0.f;
    for (int s = 0; s < NSPLIT; ++s) { float e = __expf(pp[s * (DH + 2) + DH] - M); L += e * pp[s * (DH + 2) + DH + 1]; a += e * pp[s * (DH + 2) + d]; }
    a = a / L;
    a = rbf(a);
    o[i] = a;
  }
  __syncthreads();
  int n = blockIdx.y * 64 + lane;
  float a = 0.f, f = 0.f;
  for (int k = w * 256; k < w * 256 + 256; ++k) a += o[k] * bf2f(wo[k * D + n]);
  for (int k = w * 112; k < w * 112 + 112; ++k) f += hbuf[b * F + k] * bf2f(wd[k * D + n]);
  red[w][0][lane] = a; red[w][1][lane] = f;
  __syncthreads();
  if (tid < 64) {
    float A = 0.f, Fv = 0.f;
    for (int i = 0; i < 16; ++i) { A += red[i][0][lane]; Fv += red[i][1][lane]; }
    float xv = bf2f(x[b * D + n]);
    float r = rbf(xv + rbf(A)) + rbf(Fv);
    out[b * D + n] = __float2bfloat16(r);
  }
}

extern "C" void kernel_launch(void* const* d_in, const int* in_sizes, int n_in, void* d_out, int out_size, void* d_ws, size_t ws_size,
                              hipStream_t stream) {
  const unsigned short* x = (const unsigned short*)d_in[0];
  const unsigned short* kc = (const unsigned short*)d_in[1];
  const unsigned short* vc = (const unsigned short*)d_in[2];
  const unsigned short* wq = (const unsigned short*)d_in[3];
  const unsigned short* wo = (const unsigned short*)d_in[4];
  const unsigned short* wg = (const unsigned short*)d_in[6];
  const unsigned short* wu = (const unsigned short*)d_in[7];
  const unsigned short* wd = (const unsigned short*)d_in[8];
  float* q = (float*)d_ws;
  float* hbuf = q + B * H * DH;
  float* part = hbuf + B * F;
  k1<<<dim3(B, 23), 256, 0, stream>>>(x, wq, wg, wu, q, hbuf);
  k2<<<dim3(NSPLIT, H, B), 512, 0, stream>>>(kc, vc, q, part);
  k3<<<dim3(B, 7), 1024, 0, stream>>>(x, wo, wd, part, hbuf, (__hip_bfloat16*)d_out);
}
```

```cpp
#include <hip/hip_runtime.h>
#include <stdint.h>

typedef unsigned u32x4 __attribute__((ext_vector_type(4)));
typedef unsigned u32x2 __attribute__((ext_vector_type(2)));
typedef float f32x4 __attribute__((ext_vector_type(4)));
typedef float f32x2 __attribute__((ext_vector_type(2)));
typedef __bf16 bf16x2 __attribute__((ext_vector_type(2)));
typedef __bf16 bf16x8 __attribute__((ext_vector_type(8)));
typedef __attribute__((address_space(1))) unsigned long long gu64;
typedef __amdgpu_buffer_rsrc_t rsrc_t;

constexpr int D = 448, NH = 16, DH = 256, FF = 1792;
constexpr unsigned OOB = 0x80000000u;
constexpr int NT = 2;
constexpr int DEV = 16;
constexpr int HX = 14, HI = 8, LO = 5;
constexpr int INFL = 6;
constexpr int HINFL = 3;
constexpr int LG = 3;
constexpr int PSLOTS = 32;
constexpr unsigned C0 = 950, TK = 1190, TV = 1115, PBIAS = 20, FAR = 100;
constexpr int NB = 18, NFAR = 19;
constexpr unsigned PN0 = 15, PDK = 7, PN1 = 39, PDV = 9, PLAG = 2;
constexpr unsigned GA = 100, GB = ((NB - 7) * TK + (64 - NB) * TV) / 10 + 202;
constexpr unsigned PDELAY = 700;
constexpr unsigned BG0 = 450, DTF = 50, BG1 = BG0 + 23 * DTF, DTO = 55;
constexpr unsigned WS_REGION = 262144, WS_Y = 0;
constexpr unsigned WS_Q = 8 * WS_REGION, WS_QBYTES = 8 * 16 * 2048, WS_EP = WS_Q + WS_QBYTES;
constexpr unsigned WS_PC = WS_EP + 4096, PC_STRIDE = 4352;
constexpr unsigned WS_ML = WS_PC + 131072, ML_STRIDE = 256;
constexpr unsigned long long SPIN_TICKS = 4000000ull;

struct __attribute__((aligned(16))) Lds {
  float P[4][512];
  u32x4 WQ[896];
  u32x4 XN[8][57];
  unsigned qk[128];
  float gu[2][4][64];
  float fp[8][448];
  u32x4 O[16][33];
  float ml[8];
  float fac[4];
  unsigned tag[2];
  unsigned cnt[8];
  unsigned long long pace[4];
  u32x4 KB[4][LG * 8][64];
};

__device__ __forceinline__ float rbf(float f) { return (float)(__bf16)f; }
__device__ __forceinline__ float lo2f(unsigned u) { return __uint_as_float(u << 16); }
__device__ __forceinline__ float hi2f(unsigned u) { return __uint_as_float(u & 0xffff0000u); }
__device__ __forceinline__ unsigned pkbf(float a, float b) {
  bf16x2 v = {(__bf16)a, (__bf16)b};
  return __builtin_bit_cast(unsigned, v);
}
__device__ __forceinline__ float dot2(unsigned a, unsigned b, float acc) {
  return __builtin_amdgcn_fdot2_f32_bf16(__builtin_bit_cast(bf16x2, a), __builtin_bit_cast(bf16x2, b), acc, false);
}
template <int CTRL> __device__ __forceinline__ float dpp(float x) {
  return __int_as_float(__builtin_amdgcn_update_dpp(0, __float_as_int(x), CTRL, 0xf, 0xf, true));
}
__device__ __forceinline__ float swap32_add(float a, float b) {
  auto r = __builtin_amdgcn_permlane32_swap(__float_as_uint(a), __float_as_uint(b), false, false);
  return __uint_as_float(r[0]) + __uint_as_float(r[1]);
}
__device__ __forceinline__ float swap16_add(float a, float b) {
  auto r = __builtin_amdgcn_permlane16_swap(__float_as_uint(a), __float_as_uint(b), false, false);
  return __uint_as_float(r[0]) + __uint_as_float(r[1]);
}
__device__ __forceinline__ float wave_sum(float x) {
  x += dpp<0xB1>(x); x += dpp<0x4E>(x); x += dpp<0x141>(x); x += dpp<0x140>(x);
  x = swap16_add(x, x);
  return swap32_add(x, x);
}
__device__ __forceinline__ float wave_max(float x) {
  x = fmaxf(x, dpp<0xB1>(x)); x = fmaxf(x, dpp<0x4E>(x)); x = fmaxf(x, dpp<0x141>(x)); x = fmaxf(x, dpp<0x140>(x));
  auto r = __builtin_amdgcn_permlane16_swap(__float_as_uint(x), __float_as_uint(x), false, false);
  x = fmaxf(__uint_as_float(r[0]), __uint_as_float(r[1]));
  auto q = __builtin_amdgcn_permlane32_swap(__float_as_uint(x), __float_as_uint(x), false, false);
  return fmaxf(__uint_as_float(q[0]), __uint_as_float(q[1]));
}
template <int N> __device__ __forceinline__ void wait_vm() { asm volatile("s_waitcnt vmcnt(%0)" ::"n"(N) : "memory"); }
__device__ __forceinline__ void wait_depth(unsigned lv) {
  asm volatile("s_waitcnt vmcnt(%1)\n\ts_cmp_eq_u32 %0, 0\n\ts_cbranch_scc1 .Lwd%=\n\ts_waitcnt vmcnt(%2)\n\ts_cmp_eq_u32 %0, 1\n\t"
               "s_cbranch_scc1 .Lwd%=\n\ts_waitcnt vmcnt(%3)\n.Lwd%=:" ::"s"(lv), "n"(HX - 1), "n"(HI - 1), "n"(LO - 1) : "scc", "memory");
}
__device__ __forceinline__ void wait_lds() { asm volatile("s_waitcnt lgkmcnt(0)" ::: "memory"); }
__device__ __forceinline__ void fence_c() { asm volatile("" ::: "memory"); }
__device__ __forceinline__ void sb() { __builtin_amdgcn_sched_barrier(0); }
__device__ __forceinline__ rsrc_t mk(const void* p, unsigned bytes) {
  return __builtin_amdgcn_make_buffer_rsrc((void*)p, 0, bytes, 0x00020000);
}
__device__ __forceinline__ void lds_signal(unsigned* c, int lane) {
  wait_lds();
  if (lane == 0) __hip_atomic_fetch_add(c, 1u, __ATOMIC_RELAXED, __HIP_MEMORY_SCOPE_WORKGROUP);
  fence_c();
}
template <int SLEEP> __device__ __forceinline__ void lds_wait(unsigned* c, unsigned n) {
  fence_c();
  while (__builtin_amdgcn_readfirstlane(__hip_atomic_load(c, __ATOMIC_RELAXED, __HIP_MEMORY_SCOPE_WORKGROUP)) < n)
    __builtin_amdgcn_s_sleep(SLEEP);
  fence_c();
}
__device__ __forceinline__ u32x4 pack2(float a, float b, unsigned tlo, unsigned thi) {
  u32x4 g;
  g[0] = ((__float_as_uint(a) + 0x80u) >> 8) | tlo; g[1] = thi;
  g[2] = ((__float_as_uint(b) + 0x80u) >> 8) | tlo; g[3] = thi;
  return g;
}
__device__ __forceinline__ unsigned tag_bad(u32x4 g, unsigned tlo, unsigned thi) {
  return ((g[0] ^ tlo) | (g[2] ^ tlo)) >> 24 | (g[1] ^ thi) | (g[3] ^ thi);
}
__device__ __forceinline__ f32x2 gather(rsrc_t rw, unsigned base, int lane, unsigned tlo, unsigned thi) {
  u32x4 g[4];
  const unsigned long long t0 = __builtin_amdgcn_s_memrealtime();
  for (;;) {
    fence_c();
#pragma unroll
    for (int t = 0; t < 4; ++t) g[t] = __builtin_amdgcn_raw_buffer_load_b128(rw, lane * 16 + t * 1024, base, DEV);
    unsigned bad = 0;
#pragma unroll
    for (int t = 0; t < 4; ++t) bad |= tag_bad(g[t], tlo, thi);
    if (__all(bad == 0)) break;
    if (__builtin_amdgcn_s_memrealtime() - t0 > SPIN_TICKS) break;
  }
  float s0 = 0.f, s1 = 0.f;
#pragma unroll
  for (int t = 0; t < 4; ++t) { s0 += __uint_as_float(g[t][0] << 8); s1 += __uint_as_float(g[t][2] << 8); }
  s0 += dpp<0x128>(s0); s1 += dpp<0x128>(s1);
  s0 = swap16_add(s0, s0); s1 = swap16_add(s1, s1);
  s0 = swap32_add(s0, s0); s1 = swap32_add(s1, s1);
  f32x2 r = {s0, s1};
  return r;
}
__device__ __forceinline__ float reduce8_hi(const float (&a)[8], int lane) {
  float b0 = swap32_add(a[0], a[4]), b1 = swap32_add(a[1], a[5]), b2 = swap32_add(a[2], a[6]), b3 = swap32_add(a[3], a[7]);
  float c0 = swap16_add(b0, b2), c1 = swap16_add(b1, b3);
  const bool hi = lane & 8;
  float keep = hi ? c1 : c0, send = hi ? c0 : c1;
  return keep + dpp<0x128>(send);
}

__global__ void __launch_bounds__(768, 3)
fused_decode_kernel(const unsigned char* __restrict__ x, const unsigned char* __restrict__ kc, const unsigned char* __restrict__ vc,
                    const unsigned char* __restrict__ wq, const unsigned char* __restrict__ wo, const unsigned char* __restrict__ wg,
                    const unsigned char* __restrict__ wu, const unsigned char* __restrict__ wd, unsigned* __restrict__ out,
                    unsigned char* ws) {
  __shared__ Lds sm;
  unsigned long long pc;
  unsigned clen;
  asm volatile("s_getpc_b64 %0\n.Lfd_pc:\n\ts_mov_b32 %1, (.Lfd_end-.Lfd_pc)&4294967295" : "=s"(pc), "=s"(clen));
  if (clen > 65536u) clen = 0;
  const int lane = threadIdx.x & 63;
  const int wave = __builtin_amdgcn_readfirstlane(threadIdx.x >> 6) % 12;
  const int b = blockIdx.x >> 5, j = blockIdx.x & 31;
  const int h = j & 15, sp = j >> 4;

  if (wave < 4) {
    const int w = wave;
    const size_t reg = ((size_t)((b * NH + h) * 2 + sp)) << 20;
    const rsrc_t rk = mk(kc + reg, 1u << 20), rv = mk(vc + reg, 1u << 20);
    const unsigned vbase = lane * 16 + w * 8192;
    auto ld = [&](rsrc_t r, int g, int u) -> u32x4 {
      const unsigned cu = u * 1024;
      return __builtin_amdgcn_raw_buffer_load_b128(r, vbase + (cu & 3072u), g * 32768 + (cu & ~3072u), NT);
    };
    auto dma = [&](int g, int u) {
      const unsigned cu = u * 1024;
      auto* dst = (__attribute__((address_space(3))) void*)&sm.KB[w][g * 8 + (u & 4)][0];
      const unsigned so = g * 32768 + (cu & ~3072u);
      switch (u & 3) {
        case 0: __builtin_amdgcn_raw_ptr_buffer_load_lds(rk, dst, 16, vbase, so, 0, NT); break;
        case 1: __builtin_amdgcn_raw_ptr_buffer_load_lds(rk, dst, 16, vbase, so, 1024, NT); break;
        case 2: __builtin_amdgcn_raw_ptr_buffer_load_lds(rk, dst, 16, vbase, so, 2048, NT); break;
        default: __builtin_amdgcn_raw_ptr_buffer_load_lds(rk, dst, 16, vbase, so, 3072, NT); break;
      }
    };
    u32x4 B[PSLOTS];
    unsigned sch = 0, now, ref;
    auto sample = [&]() {
      now = (unsigned)__builtin_amdgcn_s_memrealtime();
      ref = __hip_atomic_load(&sm.cnt[6], __ATOMIC_RELAXED, __HIP_MEMORY_SCOPE_WORKGROUP);
    };
    auto ahead_of = [&](int n) -> unsigned {
      const unsigned vs = now - (sch >> 4);
      const unsigned rr = __builtin_amdgcn_readfirstlane(ref);
      const unsigned a = ((vs - rr) >> 31) + (n < NFAR ? 1u : (vs - rr - FAR) >> 31);
      sch += n < NB ? TK * 16 / 10 : TV * 16 / 10;
      __hip_atomic_store(&sm.pace[w], vs | (unsigned long long)n << 32, __ATOMIC_RELAXED, __HIP_MEMORY_SCOPE_WORKGROUP);
      return a;
    };
#pragma unroll
    for (int i = 0; i < INFL; ++i) { dma(i / 8, i % 8); sb(); }
    __builtin_amdgcn_s_setprio(1);
    __builtin_amdgcn_s_barrier();
    fence_c();
#pragma unroll
    for (int i = INFL; i < LG * 8; ++i) { wait_vm<INFL - 1>(); dma(i / 8, i % 8); sb(); }
#pragma unroll
    for (int i = 0; i < PSLOTS; ++i) { wait_vm<INFL - 1>(); B[i] = ld(rk, LG + i / 8, i % 8); sb(); }
    lds_wait<1>(&sm.cnt[0], 1);
    sample();
    const u32x4 qk = *(const u32x4*)&sm.qk[(lane & 31) * 4];
    float* pw = &sm.P[w][(lane >> 5) * 8 + ((lane >> 2) & 7)];
    const bool b3 = lane & 8, b2 = lane & 4;
    auto qk_group = [&](int g, int o, bool re, rsrc_t rn, int gn) {
      float a[8];
      const unsigned ah = re ? ahead_of(gn < LG + 4 ? gn + 32 : gn) : 0;
#pragma unroll
      for (int u = 0; u < 8; ++u) {
        float d = dot2(B[o + u][0], qk[0], 0.f);
        d = dot2(B[o + u][1], qk[1], d); d = dot2(B[o + u][2], qk[2], d); d = dot2(B[o + u][3], qk[3], d);
        a[u] = d;
        if (re) { sb(); wait_depth(ah); B[o + u] = ld(rn, gn, u); if (u == 5) sample(); sb(); }
      }
      const float c0 = swap16_add(a[0], a[4]), c1 = swap16_add(a[1], a[5]), c2 = swap16_add(a[2], a[6]), c3 = swap16_add(a[3], a[7]);
      const float e0 = (b3 ? c2 : c0) + dpp<0x128>(b3 ? c0 : c2), e1 = (b3 ? c3 : c1) + dpp<0x128>(b3 ? c1 : c3);
      float s = (b2 ? e1 : e0) + dpp<0x141>(b2 ? e0 : e1);
      s += dpp<0xB1>(s); s += dpp<0x4E>(s);
      if ((lane & 3) == 0) pw[g * 16] = rbf(s) * 0.0625f;
    };
    qk_group(LG, 0, true, rk, LG + 4);
    qk_group(LG + 2, 16, false, rk, 0);
#pragma unroll
    for (int g = 0; g < LG; ++g) {
#pragma unroll
      for (int u = 0; u < 8; ++u) B[16 + u] = sm.KB[w][g * 8 + u][lane];
      qk_group(g, 16, false, rk, 0);
      if (g == 0) qk_group(LG + 1, 8, true, rk, LG + 5);
    }
    qk_group(LG + 3, 24, false, rk, 0);
    for (int g = LG + 4; g < 29; g += 2) { qk_group(g, 0, true, rk, g + 2); qk_group(g + 1, 8, true, rk, g + 3); }
    qk_group(29, 0, true, rk, 31);
    qk_group(30, 8, true, rv, 0);
    qk_group(31, 0, true, rv, 1);
    wait_lds();
    {
      f32x4 s0 = *(const f32x4*)&sm.P[w][lane * 8], s1 = *(const f32x4*)&sm.P[w][lane * 8 + 4];
      float m = fmaxf(fmaxf(fmaxf(s0[0], s0[1]), fmaxf(s0[2], s0[3])), fmaxf(fmaxf(s1[0], s1[1]), fmaxf(s1[2], s1[3])));
      m = wave_max(m);
      float l = 0.f;
#pragma unroll
      for (int i = 0; i < 4; ++i) { s0[i] = __expf(s0[i] - m); s1[i] = __expf(s1[i] - m); l += s0[i] + s1[i]; }
      *(f32x4*)&sm.P[w][lane * 8] = s0; *(f32x4*)&sm.P[w][lane * 8 + 4] = s1;
      l = wave_sum(l);
      if (lane == 0) { sm.ml[2 * w] = m; sm.ml[2 * w + 1] = l; }
      lds_signal(&sm.cnt[3], lane);
    }
    f32x2 acc[4] = {{0.f, 0.f}, {0.f, 0.f}, {0.f, 0.f}, {0.f, 0.f}};
    const float* pr = &sm.P[w][(lane >> 5) * 8];
    auto pv_group = [&](int g, int o, bool re) {
      const f32x4 p0 = *(const f32x4*)&pr[g * 16], p1 = *(const f32x4*)&pr[g * 16 + 4];
      const unsigned ah = re ? ahead_of(g + 34) : 0;
#pragma unroll
      for (int u = 0; u < 8; ++u) {
        const float p = u < 4 ? p0[u & 3] : p1[u & 3];
        const f32x2 pp = {p, p};
#pragma unroll
        for (int t = 0; t < 4; ++t) {
          const f32x2 v = {lo2f(B[o + u][t]), hi2f(B[o + u][t])};
          acc[t] = __builtin_elementwise_fma(v, pp, acc[t]);
        }
        if (re) { sb(); wait_depth(ah); B[o + u] = ld(rv, g + 2, u); if (u == 5) sample(); sb(); }
      }
    };
    for (int g = 0; g < 30; g += 2) { pv_group(g, 8, true); pv_group(g + 1, 0, true); }
    lds_wait<1>(&sm.cnt[4], 1);
    const float f = sm.fac[w];
    pv_group(30, 8, false);
    pv_group(31, 0, false);
    {
      u32x4 hi, lo;
#pragma unroll
      for (int t = 0; t < 4; ++t) {
        const float a0 = acc[t][0] * f, a1 = acc[t][1] * f;
        hi[t] = pkbf(a0, a1);
        lo[t] = pkbf(a0 - lo2f(hi[t]), a1 - hi2f(hi[t]));
      }
      const int row = w * 4 + (lane >> 5) * 2;
      sm.O[row][lane & 31] = hi; sm.O[row + 1][lane & 31] = lo;
    }
    wait_lds();
    asm volatile(".Lfd_end:");
    __builtin_amdgcn_s_barrier();
    return;
  }

  const int hw = wave - 4;
  const int r8 = lane >> 3, c8 = lane & 7;
  const rsrc_t rw = mk(ws + (size_t)b * WS_REGION, WS_REGION);
  unsigned tlo = 0, thi = 0;
  unsigned long long tg = 0;
  const unsigned long long tstart = __builtin_amdgcn_s_memrealtime();
  {
    const u32x4 xv = __builtin_amdgcn_raw_buffer_load_b128(mk(x + hw * (D * 2), D * 2), lane * 16, 0, 0);
    sb();
    unsigned pf = 0;
    if (hw == 6 && (blockIdx.x >> 3) < 4)
      pf = __builtin_amdgcn_raw_buffer_load_b32(mk((const void*)pc, clen), lane * 128 + (blockIdx.x >> 3) * 8192, 0, 0);
    sb();
    unsigned long long ep = 0;
    if (hw == 0 && lane < 8) sm.cnt[lane] = lane == 6 ? (unsigned)tstart + C0 : lane == 7 ? (unsigned)tstart : 0u;
    if (hw == 0 && lane < 4) sm.pace[lane] = 0;
    if (hw == 7) {
      asm volatile("s_load_dwordx2 %0, %1, 0x0" : "=s"(ep) : "s"(ws + WS_EP) : "memory");
      const rsrc_t rq = mk(wq, D * NH * DH * 2);
      const unsigned vq = (lane >> 1) * 8192 + (lane & 1) * 16, sq = h * 512 + (b * 2 + sp) * 32;
#pragma unroll
      for (int i = 0; i < 14; ++i)
        __builtin_amdgcn_raw_ptr_buffer_load_lds(rq, (__attribute__((address_space(3))) void*)&sm.WQ[i * 64], 16, vq, sq + i * (32 * 8192), 0, NT);
      asm volatile("s_waitcnt lgkmcnt(0)" : "+s"(ep)::"memory");
    }
    wait_lds();
    sb();
    __builtin_amdgcn_s_barrier();
    sb();
    float v[8];
#pragma unroll
    for (int t = 0; t < 4; ++t) { v[2 * t] = lo2f(xv[t]); v[2 * t + 1] = hi2f(xv[t]); }
    float s = ((v[0] + v[1]) + (v[2] + v[3])) + ((v[4] + v[5]) + (v[6] + v[7]));
    const float mean = wave_sum(s) * (1.f / D);
    float q2 = 0.f;
#pragma unroll
    for (int t = 0; t < 8; ++t) { const float d = v[t] - mean; q2 += d * d; }
    if (lane >= 56) q2 = 0.f;
    const float var = wave_sum(q2) * (1.f / D);
    const float mb = rbf(mean);
    const float rden = __builtin_amdgcn_rcpf(rbf(__builtin_amdgcn_sqrtf(rbf(rbf(var) + 1e-5f))));
    u32x4 xn;
#pragma unroll
    for (int t = 0; t < 4; ++t) xn[t] = pkbf(rbf(v[2 * t] - mb) * rden, rbf(v[2 * t + 1] - mb) * rden);
    if (lane < 56) sm.XN[hw][lane] = xn;
    lds_signal(&sm.cnt[5], lane);
    asm volatile("" ::"v"(pf));
    if (hw == 7) {
      wait_vm<0>();
      typedef short s16x4 __attribute__((ext_vector_type(4)));
      typedef __attribute__((address_space(3))) s16x4* lds4_t;
      const unsigned char* wb = (const unsigned char*)sm.WQ + (lane >> 4) * 256 + ((lane & 15) >> 2) * 32 + (lane & 3) * 8;
      u32x4 A[14];
#pragma unroll
      for (int i = 0; i < 14; ++i) {
        const u32x2 lo = __builtin_bit_cast(u32x2, __builtin_amdgcn_ds_read_tr16_b64_v4i16((lds4_t)(wb + i * 1024)));
        const u32x2 hi = __builtin_bit_cast(u32x2, __builtin_amdgcn_ds_read_tr16_b64_v4i16((lds4_t)(wb + i * 1024 + 128)));
        A[i][0] = lo[0]; A[i][1] = lo[1]; A[i][2] = hi[0]; A[i][3] = hi[1];
      }
      tg = ep + 1;
      tlo = (unsigned)tg << 24; thi = (unsigned)(tg >> 8);
      lds_wait<0>(&sm.cnt[5], 8);
      f32x4 c0 = {0.f, 0.f, 0.f, 0.f}, c1 = {0.f, 0.f, 0.f, 0.f};
#pragma unroll
      for (int i = 0; i < 14; i += 2) {
        c0 = __builtin_amdgcn_mfma_f32_16x16x32_bf16(__builtin_bit_cast(bf16x8, A[i]), __builtin_bit_cast(bf16x8, sm.XN[lane & 7][i * 4 + (lane >> 4)]), c0, 0, 0, 0);
        c1 = __builtin_amdgcn_mfma_f32_16x16x32_bf16(__builtin_bit_cast(bf16x8, A[i + 1]), __builtin_bit_cast(bf16x8, sm.XN[lane & 7][i * 4 + 4 + (lane >> 4)]), c1, 0, 0, 0);
      }
      c0 += c1;
      const bool up = lane & 8;
      const rsrc_t rqi = mk(ws + WS_Q, WS_QBYTES);
      const unsigned t4 = (unsigned)tstart >> 2;
      u32x4 qm = pack2(rbf(up ? c0[2] : c0[0]), rbf(up ? c0[3] : c0[1]), tlo, thi);
      qm[0] |= t4 & 0xffu;
      __builtin_amdgcn_raw_buffer_store_b128(qm, rqi,
                                             (lane & 7) * (16 * 2048) + (lane >> 4) * 32 + (lane & 8) * 2, h * 2048 + (b * 2 + sp) * 128, DEV);
      const unsigned qo = (b * 16 + h) * 2048;
      while (__builtin_amdgcn_s_memrealtime() - tstart < PDELAY) __builtin_amdgcn_s_sleep(2);
      u32x4 r0, r1;
      for (;;) {
        fence_c();
        r0 = __builtin_amdgcn_raw_buffer_load_b128(rqi, lane * 16, qo, DEV);
        r1 = __builtin_amdgcn_raw_buffer_load_b128(rqi, lane * 16 + 1024, qo, DEV);
        if (__all((tag_bad(r0, tlo, thi) | tag_bad(r1, tlo, thi)) == 0) || __builtin_amdgcn_s_memrealtime() - tstart > SPIN_TICKS) break;
        __builtin_amdgcn_s_sleep(4);
      }
      sm.qk[lane] = __builtin_amdgcn_perm(r0[2], r0[0], 0x06050201u);
      sm.qk[64 + lane] = __builtin_amdgcn_perm(r1[2], r1[0], 0x06050201u);
      lds_signal(&sm.cnt[0], lane);
      const float dl = wave_max(fmaxf((float)(int)(signed char)(r0[0] - t4), (float)(int)(signed char)(r1[0] - t4)));
      if (lane == 0) __hip_atomic_store(&sm.cnt[7], (t4 + (int)dl) << 2, __ATOMIC_RELAXED, __HIP_MEMORY_SCOPE_WORKGROUP);
    } else {
      lds_wait<2>(&sm.cnt[5], 8);
    }
  }
  const unsigned short* xr = (const unsigned short*)sm.XN[b];
  auto pace_wait = [&](unsigned n) -> unsigned {
    unsigned long long me;
    for (;;) {
      fence_c();
      me = __hip_atomic_load(&sm.pace[lane & 3], __ATOMIC_RELAXED, __HIP_MEMORY_SCOPE_WORKGROUP);
      if (__all((unsigned)(me >> 32) >= n)) break;
      __builtin_amdgcn_s_sleep(8);
    }
    return (unsigned)me;
  };
  auto pace_step = [&](unsigned n) {
    const rsrc_t rp = mk(ws + WS_PC, 16 * PC_STRIDE);
    const unsigned base = (unsigned)tstart;
    if (j < 16 && b == ((j ^ (j >> 3) * 5) & 7)) {
      float d = (float)(int)(pace_wait(n) - base);
      d += dpp<0xB1>(d); d += dpp<0x4E>(d);
      const u32x2 m = {((base + (int)(d * 0.25f)) & 0xffffffu) | tlo, thi};
      __builtin_amdgcn_raw_buffer_store_b64(m, rp, lane < 16 ? lane * 8 : OOB, j * PC_STRIDE, DEV);
    }
    pace_wait(n + PLAG);
    const u32x2 g = __builtin_amdgcn_raw_buffer_load_b64(rp, lane < 16 ? lane * PC_STRIDE : OOB, 0, DEV);
    const bool ok = lane < 16 && (((g[0] ^ tlo) >> 24) | (g[1] ^ thi)) == 0;
    const int c = __popcll(__ballot(ok));
    float d = ok ? (float)((int)((g[0] - base) << 8) >> 8) : 0.f;
    d += dpp<0xB1>(d); d += dpp<0x4E>(d); d += dpp<0x141>(d); d += dpp<0x140>(d);
    if (c >= 12 && lane == 0) __hip_atomic_store(&sm.cnt[6], base + (int)(d * __builtin_amdgcn_rcpf((float)c)) + PBIAS, __ATOMIC_RELAXED, __HIP_MEMORY_SCOPE_WORKGROUP);
    fence_c();
  };

  auto bg_base = [&]() -> unsigned {
    return __builtin_amdgcn_readfirstlane(__hip_atomic_load(&sm.cnt[7], __ATOMIC_RELAXED, __HIP_MEMORY_SCOPE_WORKGROUP));
  };
  auto bg_wait = [&](unsigned t) {
    while ((int)((unsigned)__builtin_amdgcn_s_memrealtime() - t) < 0) __builtin_amdgcn_s_sleep(2);
  };

  if (j < 28) {
    const int rq4 = hw >> 1;
    const rsrc_t rg = mk((hw & 1) ? wu : wg, D * FF * 2);
    const unsigned vg = r8 * (FF * 2) + c8 * 16, sg = rq4 * (112 * FF * 2) + j * 128;
    u32x4 W[14];
#pragma unroll
    for (int i = 0; i < 14; ++i) {
      if (i >= HINFL) wait_vm<HINFL - 1>();
      bg_wait(bg_base() + BG0 + i * DTF);
      W[i] = __builtin_amdgcn_raw_buffer_load_b128(rg, vg, sg + i * (8 * FF * 2), NT); sb();
    }
    const rsrc_t rd = mk(wd, FF * D * 2);
    const unsigned vd = lane < 56 ? lane * 16 : OOB;
    u32x4 Wd[8];
#pragma unroll
    for (int i = 0; i < 8; ++i) {
      wait_vm<HINFL - 1>();
      bg_wait(bg_base() + BG0 + (14 + i) * DTF);
      Wd[i] = __builtin_amdgcn_raw_buffer_load_b128(rd, vd, (j * 64 + hw * 8 + i) * (D * 2), NT); sb();
    }
    float a[8] = {0.f, 0.f, 0.f, 0.f, 0.f, 0.f, 0.f, 0.f};
#pragma unroll
    for (int i = 0; i < 14; ++i) {
      const unsigned xl = xr[(rq4 * 14 + i) * 8 + r8], xh = xl << 16;
#pragma unroll
      for (int t = 0; t < 4; ++t) { a[2 * t] = dot2(W[i][t], xl, a[2 * t]); a[2 * t + 1] = dot2(W[i][t], xh, a[2 * t + 1]); }
    }
    sm.gu[hw & 1][rq4][c8 * 8 + r8] = reduce8_hi(a, lane);
    lds_signal(&sm.cnt[1], lane);
    lds_wait<4>(&sm.cnt[1], 8);
    const float gg = rbf((sm.gu[0][0][lane] + sm.gu[0][1][lane]) + (sm.gu[0][2][lane] + sm.gu[0][3][lane]));
    const float uu = rbf((sm.gu[1][0][lane] + sm.gu[1][1][lane]) + (sm.gu[1][2][lane] + sm.gu[1][3][lane]));
    const float sgm = rbf(1.f / (1.f + __expf(-gg)));
    const unsigned hb = __float_as_uint(rbf(sgm * uu));
#pragma unroll
    for (int t = 0; t < 8; ++t) a[t] = 0.f;
#pragma unroll
    for (int i = 0; i < 8; ++i) {
      const unsigned xh = __builtin_amdgcn_readlane(hb, hw * 8 + i), xl = xh >> 16;
#pragma unroll
      for (int t = 0; t < 4; ++t) { a[2 * t] = dot2(Wd[i][t], xl, a[2 * t]); a[2 * t + 1] = dot2(Wd[i][t], xh, a[2 * t + 1]); }
    }
    if (lane < 56) {
      const f32x4 o0 = {a[0], a[1], a[2], a[3]}, o1 = {a[4], a[5], a[6], a[7]};
      *(f32x4*)&sm.fp[hw][lane * 8] = o0; *(f32x4*)&sm.fp[hw][lane * 8 + 4] = o1;
    }
    lds_signal(&sm.cnt[2], lane);
  }

  if (hw < 7) {
    const int g4 = lane >> 4, n = lane & 15;
    const rsrc_t ro = mk(wo, NH * DH * D * 2);
    const unsigned vo = g4 * (8 * D * 2) + n * 8, so = h * (DH * D * 2) + hw * 128;
    u32x4 Wf[8][4];
#pragma unroll
    for (int kb = 0; kb < 8; ++kb) {
      u32x2 r[8];
      const unsigned bb = bg_base();
#pragma unroll
      for (int i = 0; i < 8; ++i) {
        wait_vm<2 * HINFL - 1>();
        bg_wait(bb + BG1 + (kb * 8 + i) * DTO);
        r[i] = __builtin_amdgcn_raw_buffer_load_b64(ro, vo, so + (kb * 32 + i) * (D * 2), NT); sb();
      }
#pragma unroll
      for (int d = 0; d < 4; ++d) {
        Wf[kb][0][d] = __builtin_amdgcn_perm(r[2 * d + 1][0], r[2 * d][0], 0x05040100u);
        Wf[kb][1][d] = __builtin_amdgcn_perm(r[2 * d + 1][0], r[2 * d][0], 0x07060302u);
        Wf[kb][2][d] = __builtin_amdgcn_perm(r[2 * d + 1][1], r[2 * d][1], 0x05040100u);
        Wf[kb][3][d] = __builtin_amdgcn_perm(r[2 * d + 1][1], r[2 * d][1], 0x07060302u);
      }
      asm volatile("" : "+v"(Wf[kb][0]), "+v"(Wf[kb][1]), "+v"(Wf[kb][2]), "+v"(Wf[kb][3]));
      sb();
    }
    const int col = hw * 64 + n * 4 + (lane >> 5) * 2;
    const unsigned vy = (lane & 16) ? OOB : (unsigned)((col >> 4) * 4096 + (col & 15) * 8);
    f32x2 fs = {0.f, 0.f};
    if (j < 28) {
      lds_wait<4>(&sm.cnt[2], 8);
#pragma unroll
      for (int i = 0; i < 8; ++i) fs += *(const f32x2*)&sm.fp[i][col];
    }
    fence_c();
    __builtin_amdgcn_s_barrier();
    fence_c();
    u32x4 A[8];
#pragma unroll
    for (int kb = 0; kb < 8; ++kb) A[kb] = sm.O[n][kb * 4 + g4];
    tlo = sm.tag[0]; thi = sm.tag[1];
    f32x4 c[4] = {{0.f, 0.f, 0.f, 0.f}, {0.f, 0.f, 0.f, 0.f}, {0.f, 0.f, 0.f, 0.f}, {0.f, 0.f, 0.f, 0.f}};
#pragma unroll
    for (int kb = 0; kb < 8; ++kb)
#pragma unroll
      for (int i = 0; i < 4; ++i)
        c[i] = __builtin_amdgcn_mfma_f32_16x16x32_bf16(__builtin_bit_cast(bf16x8, A[kb]), __builtin_bit_cast(bf16x8, Wf[kb][i]), c[i], 0, 0, 0);
    float y[4];
#pragma unroll
    for (int i = 0; i < 4; ++i) y[i] = (c[i][0] + c[i][1]) + (c[i][2] + c[i][3]);
    float y0 = swap32_add(y[0], y[2]), y1 = swap32_add(y[1], y[3]);
    y0 = swap16_add(y0, y0) + fs[0]; y1 = swap16_add(y1, y1) + fs[1];
    __builtin_amdgcn_raw_buffer_store_b128(pack2(y0, y1, tlo, thi), rw, vy, WS_Y + j * 128, DEV);
    return;
  }

  if (lane == 0) { sm.tag[0] = tlo; sm.tag[1] = thi; }
  const unsigned xo = __builtin_amdgcn_raw_buffer_load_b32(mk(x + b * (D * 2), D * 2), j * 32 + c8 * 4, 0, 0);
  {
    for (unsigned n = PN0; n + PLAG < 34; n += PDK) pace_step(n);
    lds_wait<16>(&sm.cnt[3], 4);
    const float mw = sm.ml[(lane & 3) * 2], lw = sm.ml[(lane & 3) * 2 + 1];
    float m = fmaxf(mw, dpp<0xB1>(mw)); m = fmaxf(m, dpp<0x4E>(m));
    float l = lw * __expf(mw - m); l += dpp<0xB1>(l); l += dpp<0x4E>(l);
    const u32x4 mine = pack2(m, l, tlo, thi);
    const rsrc_t rm = mk(ws + WS_ML, 256 * ML_STRIDE);
    __builtin_amdgcn_raw_buffer_store_b128(mine, rm, lane == 0 ? 0u : OOB, (blockIdx.x ^ 16) * ML_STRIDE, DEV);
    for (unsigned n = PN1; n < PN1 + 2 * PDV; n += PDV) pace_step(n);
    u32x4 g;
    const unsigned long long t0 = __builtin_amdgcn_s_memrealtime();
    for (;;) {
      fence_c();
      g = __builtin_amdgcn_raw_buffer_load_b128(rm, lane < 4 ? 0u : OOB, blockIdx.x * ML_STRIDE, DEV);
      if (__all(tag_bad(g, tlo, thi) == 0 || lane >= 4)) break;
      if (__builtin_amdgcn_s_memrealtime() - t0 > SPIN_TICKS) break;
      __builtin_amdgcn_s_sleep(16);
    }
    const float m0 = __uint_as_float(mine[0] << 8), l0 = __uint_as_float(mine[2] << 8);
    const float m1 = __uint_as_float(g[0] << 8), l1 = __uint_as_float(g[2] << 8);
    const float mm = fmaxf(m0, m1);
    const float lt = l0 * __expf(m0 - mm) + l1 * __expf(m1 - mm);
    if (lane < 4) sm.fac[lane] = __expf(mw - mm) / lt;
    lds_signal(&sm.cnt[4], lane);
  }
  pace_step(PN1 + 2 * PDV);
  wait_lds();
  __builtin_amdgcn_s_barrier();
  fence_c();
  if (j >= 28) return;
  {
    const unsigned ta = (unsigned)__builtin_amdgcn_s_memrealtime() + GA;
    const unsigned tb = __builtin_amdgcn_readfirstlane(__hip_atomic_load(&sm.cnt[6], __ATOMIC_RELAXED, __HIP_MEMORY_SCOPE_WORKGROUP)) + GB;
    const int dl = (int)(tb - ta);
    const unsigned tt = dl > 0 && dl < 150 ? tb : ta;
    while ((int)((unsigned)__builtin_amdgcn_s_memrealtime() - tt) < 0) __builtin_amdgcn_s_sleep(1);
  }
  const f32x2 aa = gather(rw, WS_Y + j * 4096, lane, tlo, thi);
  __builtin_amdgcn_raw_buffer_store_b32(pkbf(lo2f(xo) + aa[0], hi2f(xo) + aa[1]), mk(out, 8 * D * 2), lane < 8 ? lane * 4 : OOB, b * (D * 2) + j * 32, DEV);
  if (lane == 0 && blockIdx.x == 0) __hip_atomic_store((gu64*)(ws + WS_EP), tg, __ATOMIC_RELAXED, __HIP_MEMORY_SCOPE_AGENT);
}

extern "C" void kernel_launch(void* const* d_in, const int* in_sizes, int n_in, void* d_out, int out_size, void* d_ws, size_t ws_size,
                              hipStream_t stream) {
  fused_decode_kernel<<<256, 768, 0, stream>>>((const unsigned char*)d_in[0], (const unsigned char*)d_in[1], (const unsigned char*)d_in[2],
                                               (const unsigned char*)d_in[3], (const unsigned char*)d_in[4], (const unsigned char*)d_in[6],
                                               (const unsigned char*)d_in[7], (const unsigned char*)d_in[8], (unsigned*)d_out,
                                               (unsigned char*)d_ws);
}
```
